```python
import torch

def _softmax(x, axis=-1):
    return torch.softmax(x, dim=axis)
def _gelu(x):
    return torch.nn.functional.gelu(x, approximate="tanh")
def _silu(x):
    return torch.nn.functional.silu(x)
def _sigmoid(x):
    return torch.sigmoid(x)
def _layernorm(x, axis=-1, eps=1e-5):
    m = x.mean(dim=axis, keepdim=True)
    v = x.var(dim=axis, keepdim=True, unbiased=False)
    return (x - m) / torch.sqrt(v + eps)
def _scatter_add(tgt, idx, src, axis=0):
    return tgt.clone().index_add_(axis, idx, src)
def _segment_sum(src, seg, n):
    out = torch.zeros((n,) + src.shape[1:], dtype=src.dtype)
    return out.index_add_(0, seg, src)
def _pad(x, pw):
    flat = tuple(v for pair in reversed(pw) for v in pair)
    return torch.nn.functional.pad(x, flat)

kernel_name = 'interleave_layers__sliding_local_dilated__glu_gated_swiglu__prompt'

def setup_inputs():

    _w = (lambda a: (lambda t: t.to(torch.bfloat16) if t.is_floating_point() else t)(torch.as_tensor(a)))
    def _A_setup():
        B, S, D = 1, 2048, 1024
        return {
            "x": _w((lambda *s: torch.randn(*s, dtype=torch.bfloat16) * 0.05)(B, S, D)),
            "wqkv": _w((lambda *s: torch.randn(*s, dtype=torch.bfloat16) * 0.05)(D, 3 * D)),
            "wo": _w((lambda *s: torch.randn(*s, dtype=torch.bfloat16) * 0.05)(D, D)),
        }
    _A_in = _A_setup()

    def _B_setup():
        B, S, D, F = 1, 2048, 1024, 3584
        return {
            "x": _w((lambda *s: torch.randn(*s, dtype=torch.bfloat16) * 0.05)(B, S, D)),
            "w1": _w((lambda *s: torch.randn(*s, dtype=torch.bfloat16) * 0.8)(D, F) * (2.0 / D) ** 0.5),
            "w3": _w((lambda *s: torch.randn(*s, dtype=torch.bfloat16) * 0.8)(D, F) * (2.0 / D) ** 0.5),
            "w2": _w((lambda *s: torch.randn(*s, dtype=torch.bfloat16) * 0.8)(F, D) * (2.0 / F) ** 0.5),
        }
    _B_in = _B_setup()

    return {
        "A_x": _A_in["x"],
        "A_wqkv": _A_in["wqkv"],
        "A_wo": _A_in["wo"],
        "B_x": _B_in["x"],
        "B_w1": _B_in["w1"],
        "B_w3": _B_in["w3"],
        "B_w2": _B_in["w2"],
    }

def reference(A_x, A_wqkv, A_wo, B_x, B_w1, B_w3, B_w2):
    def _A_ref(x, wqkv, wo):
        B, S, D = x.shape
        H = 32; Dh = D // H
        W, dil = 8, 2
        qkv = (lambda x, *d: x.permute(*d))((x @ wqkv).reshape(B, S, 3, H, Dh), 2, 0, 3, 1, 4)
        q, k, v = qkv[0], qkv[1], qkv[2]
        scale = Dh ** -0.5
        pos = torch.arange(S)
        off = torch.arange(W) * dil
        idx = pos[:, None] - off[None, :]
        mask = idx >= 0
        idx_c = torch.where(mask, idx, 0).reshape(-1)
        kg = k[:, :, idx_c, :].reshape(B, H, S, W, Dh)
        vg = v[:, :, idx_c, :].reshape(B, H, S, W, Dh)
        logits = torch.einsum("bhsd,bhswd->bhsw", q, kg) * scale
        logits = torch.where(mask[None, None, :, :], logits, -1e9)
        attn = _softmax(logits, axis=-1)
        o = torch.einsum("bhsw,bhswd->bhsd", attn, vg)
        return (lambda x, *d: x.permute(*d))(o, 0, 2, 1, 3).reshape(B, S, D) @ wo

    def _B_ref(x, w1, w3, w2):
        gate = torch.einsum("bsd,df->bsf", x, w1)
        up = torch.einsum("bsd,df->bsf", x, w3)
        h = _silu(gate) * up
        out = torch.einsum("bsf,fd->bsd", h, w2)
        return out

    x = A_x
    x = x + _A_ref(_layernorm(x, axis=-1), A_wqkv, A_wo)
    x = x + _B_ref(_layernorm(x, axis=-1), B_w1, B_w3, B_w2)
    x = x + _A_ref(_layernorm(x, axis=-1), A_wqkv, A_wo)
    x = x + _B_ref(_layernorm(x, axis=-1), B_w1, B_w3, B_w2)
    return x
```

```cpp
#include <hip/hip_runtime.h>
#include <hip/hip_bf16.h>
#include <stdint.h>

typedef unsigned short u16;
typedef short s16x8 __attribute__((ext_vector_type(8)));
typedef short s16x4 __attribute__((ext_vector_type(4)));
typedef float f32x4 __attribute__((ext_vector_type(4)));
typedef unsigned u32x4 __attribute__((ext_vector_type(4)));
typedef unsigned u32x2 __attribute__((ext_vector_type(2)));
#define LDS_AS __attribute__((address_space(3)))

constexpr int SEQ = 2048, DM = 1024, FF = 3584, NH = 32, DHD = 32;

__device__ __forceinline__ float bf2f(u16 v) { return __uint_as_float(((unsigned)v) << 16); }
__device__ __forceinline__ u16 f2bf(float f) {
  __bf16 b = (__bf16)f;
  return __builtin_bit_cast(u16, b);
}
__device__ __forceinline__ float rbf(float f) { return bf2f(f2bf(f)); }

__global__ __launch_bounds__(256) void ln_k(const u16* __restrict__ x, u16* __restrict__ y) {
  const int row = blockIdx.x * 4 + (threadIdx.x >> 6);
  const int lane = threadIdx.x & 63;
  const u16* xr = x + (size_t)row * DM;
  u32x4 v0 = *(const u32x4*)(xr + lane * 8);
  u32x4 v1 = *(const u32x4*)(xr + 512 + lane * 8);
  float f[16];
#pragma unroll
  for (int i = 0; i < 4; ++i) {
    f[2 * i] = __uint_as_float(v0[i] << 16);
    f[2 * i + 1] = __uint_as_float(v0[i] & 0xffff0000u);
    f[8 + 2 * i] = __uint_as_float(v1[i] << 16);
    f[8 + 2 * i + 1] = __uint_as_float(v1[i] & 0xffff0000u);
  }
  float s = 0.f;
#pragma unroll
  for (int i = 0; i < 16; ++i) s += f[i];
#pragma unroll
  for (int o = 32; o > 0; o >>= 1) s += __shfl_xor(s, o);
  const float mean = s * (1.0f / DM);
  float q = 0.f;
#pragma unroll
  for (int i = 0; i < 16; ++i) { float d = f[i] - mean; q += d * d; }
#pragma unroll
  for (int o = 32; o > 0; o >>= 1) q += __shfl_xor(q, o);
  const float var = q * (1.0f / DM);
  const float mb = rbf(mean);
  const float sd = rbf(sqrtf(rbf(rbf(var) + 1e-5f)));
  u32x4 o0, o1;
#pragma unroll
  for (int i = 0; i < 4; ++i) {
    u16 a = f2bf(rbf(f[2 * i] - mb) / sd), b = f2bf(rbf(f[2 * i + 1] - mb) / sd);
    u16 c = f2bf(rbf(f[8 + 2 * i] - mb) / sd), d = f2bf(rbf(f[8 + 2 * i + 1] - mb) / sd);
    o0[i] = (unsigned)a | ((unsigned)b << 16);
    o1[i] = (unsigned)c | ((unsigned)d << 16);
  }
  u16* yr = y + (size_t)row * DM;
  *(u32x4*)(yr + lane * 8) = o0;
  *(u32x4*)(yr + 512 + lane * 8) = o1;
}

__global__ __launch_bounds__(256) void attn_k(const u16* __restrict__ qkv, u16* __restrict__ o) {
  const int gid = blockIdx.x * 256 + threadIdx.x;
  const int sub = gid & 3, pair = gid >> 2;
  const int h = pair & 31, s = pair >> 5;
  const u16* base = qkv + h * 32 + sub * 8;
  u32x4 qv = *(const u32x4*)(base + (size_t)s * 3072);
  float q[8];
#pragma unroll
  for (int i = 0; i < 4; ++i) { q[2 * i] = __uint_as_float(qv[i] << 16); q[2 * i + 1] = __uint_as_float(qv[i] & 0xffff0000u); }
  float lg[8];
  const float scale = 0.17677669529663687f;
#pragma unroll
  for (int j = 0; j < 8; ++j) {
    const int idx = s - 2 * j;
    float d = 0.f;
    if (idx >= 0) {
      u32x4 kv = *(const u32x4*)(base + (size_t)idx * 3072 + 1024);
#pragma unroll
      for (int i = 0; i < 4; ++i) {
        d += q[2 * i] * __uint_as_float(kv[i] << 16);
        d += q[2 * i + 1] * __uint_as_float(kv[i] & 0xffff0000u);
      }
    }
    d += __shfl_xor(d, 1);
    d += __shfl_xor(d, 2);
    lg[j] = (idx >= 0) ? rbf(rbf(d) * scale) : -1.0e9f;
  }
  float mx = lg[0];
#pragma unroll
  for (int j = 1; j < 8; ++j) mx = fmaxf(mx, lg[j]);
  float e[8], sum = 0.f;
#pragma unroll
  for (int j = 0; j < 8; ++j) { e[j] = (s - 2 * j >= 0) ? expf(lg[j] - mx) : 0.f; sum += e[j]; }
  const float inv = 1.0f / sum;
  float acc[8];
#pragma unroll
  for (int i = 0; i < 8; ++i) acc[i] = 0.f;
#pragma unroll
  for (int j = 0; j < 8; ++j) {
    const int idx = s - 2 * j;
    if (idx >= 0) {
      const float p = rbf(e[j] * inv);
      u32x4 vv = *(const u32x4*)(base + (size_t)idx * 3072 + 2048);
#pragma unroll
      for (int i = 0; i < 4; ++i) {
        acc[2 * i] += p * __uint_as_float(vv[i] << 16);
        acc[2 * i + 1] += p * __uint_as_float(vv[i] & 0xffff0000u);
      }
    }
  }
  u32x4 ov;
#pragma unroll
  for (int i = 0; i < 4; ++i) ov[i] = (unsigned)f2bf(acc[2 * i]) | ((unsigned)f2bf(acc[2 * i + 1]) << 16);
  *(u32x4*)(o + (size_t)s * DM + h * 32 + sub * 8) = ov;
}

__device__ __forceinline__ s16x8 tr_frag(const char* p) {
  s16x4 lo = __builtin_amdgcn_ds_read_tr16_b64_v4i16((LDS_AS s16x4*)(p));
  s16x4 hi = __builtin_amdgcn_ds_read_tr16_b64_v4i16((LDS_AS s16x4*)(p + 512));
  s16x8 r;
  r[0] = lo[0]; r[1] = lo[1]; r[2] = lo[2]; r[3] = lo[3];
  r[4] = hi[0]; r[5] = hi[1]; r[6] = hi[2]; r[7] = hi[3];
  return r;
}

template <int BM, int BN, int WM, int WN, int NST, int EPI, int PA, int PB, int NPB>
__global__ __launch_bounds__(WM * WN * 64, (WM * WN * 64) / 256)
void gemm_k(const u16* __restrict__ A, const u16* __restrict__ B0, const u16* __restrict__ B1,
            const u16* __restrict__ R, u16* __restrict__ C, int K, int ldb, int ldc) {
  constexpr int NW = WM * WN, NTH = NW * 64;
  constexpr int MT = BM / (WM * 16), NT = BN / (WN * 16);
  constexpr int ABYTES = BM * 128, BBYTES = BN * 128, SBYTES = ABYTES + BBYTES;
  constexpr int APW = BM / 8 / NW, BPW = BN / 8 / NW, PPW = APW + BPW;
  constexpr int BNO = (EPI == 2) ? BN / 2 : BN;
  constexpr int CSTRIDE = BNO * 2 + 16;
  constexpr int SMEM = (NST * SBYTES > BM * CSTRIDE) ? NST * SBYTES : BM * CSTRIDE;
  __shared__ __attribute__((aligned(16))) char smem[SMEM];

  const int tid = threadIdx.x, lane = tid & 63;
  const int wave = __builtin_amdgcn_readfirstlane(tid >> 6);
  const int wm = wave / WN, wn = wave % WN;
  const int bx = blockIdx.x & 7, bi = blockIdx.x >> 3;
  const int tm = (bx / NPB) * PA + bi / PB, tn = (bx % NPB) * PB + bi % PB;
  const int m0 = tm * BM, n0 = tn * BNO;

  const u16* aptr[APW];
  const u16* bptr[BPW];
#pragma unroll
  for (int j = 0; j < APW; ++j) {
    const int row = (wave * APW + j) * 8 + (lane >> 3);
    aptr[j] = A + (size_t)(m0 + row) * K + (((lane & 7) ^ (lane >> 3)) << 3);
  }
#pragma unroll
  for (int j = 0; j < BPW; ++j) {
    const int i = wave * BPW + j;
    const int nb = i >> 3, kr = ((i & 7) << 3) + (lane >> 3);
    const int chunk = (lane & 7) ^ ((((lane >> 4) & 1) | ((i & 1) << 1)) << 1);
    const u16* bsel = B0;
    int col = n0 + nb * 64;
    if (EPI == 2 && nb >= BN / 128) { bsel = B1; col = n0 + (nb - BN / 128) * 64; }
    bptr[j] = bsel + (size_t)kr * ldb + col + chunk * 8;
  }
  const size_t bstep = (size_t)64 * ldb;

  auto issue = [&](int buf) {
    char* sa = smem + buf * SBYTES;
#pragma unroll
    for (int j = 0; j < APW; ++j) {
      __builtin_amdgcn_global_load_lds((const unsigned*)aptr[j], (LDS_AS unsigned*)(sa + (wave * APW + j) * 1024), 16, 0, 0);
      aptr[j] += 64;
    }
#pragma unroll
    for (int j = 0; j < BPW; ++j) {
      __builtin_amdgcn_global_load_lds((const unsigned*)bptr[j], (LDS_AS unsigned*)(sa + ABYTES + (wave * BPW + j) * 1024), 16, 0, 0);
      bptr[j] += bstep;
    }
  };

  int aoff[2];
#pragma unroll
  for (int ks = 0; ks < 2; ++ks)
    aoff[ks] = (wm * MT * 16 + (lane & 15)) * 128 + (((ks * 4 + (lane >> 4)) ^ (lane & 7)) << 4);
  int boff[NT];
  {
    const int hh = ((lane >> 3) & 1) | (((lane >> 4) & 1) << 1);
    const int krow = 8 * (lane >> 4) + ((lane & 15) >> 2);
#pragma unroll
    for (int nt = 0; nt < NT; ++nt) {
      int n16, nb;
      if (EPI == 2) { n16 = wn * (NT / 2) + (nt % (NT / 2)); nb = (nt / (NT / 2)) * (BN / 128) + (n16 >> 2); }
      else { n16 = wn * NT + nt; nb = n16 >> 2; }
      const int cg = n16 & 3;
      boff[nt] = ABYTES + nb * 8192 + krow * 128 + (((((cg ^ hh) << 1) | ((lane >> 1) & 1))) << 4) + ((lane & 1) << 3);
    }
  }

  f32x4 acc[MT][NT];
#pragma unroll
  for (int mt = 0; mt < MT; ++mt)
#pragma unroll
    for (int nt = 0; nt < NT; ++nt) acc[mt][nt] = (f32x4){0.f, 0.f, 0.f, 0.f};

  const int nk = K >> 6;
#pragma unroll
  for (int s = 0; s < NST - 1; ++s) issue(s);
  int rbuf = 0, wbuf = NST - 1;
  for (int kt = 0; kt < nk; ++kt) {
    if (kt + NST - 1 <= nk) {
      if constexpr (NST == 2) asm volatile("s_waitcnt vmcnt(0)" ::: "memory");
      else if constexpr (PPW * (NST - 2) == 6) asm volatile("s_waitcnt vmcnt(6)" ::: "memory");
      else if constexpr (PPW * (NST - 2) == 8) asm volatile("s_waitcnt vmcnt(8)" ::: "memory");
      else if constexpr (PPW * (NST - 2) == 10) asm volatile("s_waitcnt vmcnt(10)" ::: "memory");
      else if constexpr (PPW * (NST - 2) == 12) asm volatile("s_waitcnt vmcnt(12)" ::: "memory");
      else if constexpr (PPW * (NST - 2) == 20) asm volatile("s_waitcnt vmcnt(20)" ::: "memory");
      else static_assert(NST == 2, "add vmcnt case");
    } else {
      asm volatile("s_waitcnt vmcnt(0)" ::: "memory");
    }
    asm volatile("s_waitcnt lgkmcnt(0)" ::: "memory");
    __builtin_amdgcn_s_barrier();
    asm volatile("" ::: "memory");
    if (kt + NST - 1 < nk) { issue(wbuf); }
    wbuf = (wbuf + 1 == NST) ? 0 : wbuf + 1;
    const char* sb = smem + rbuf * SBYTES;
    rbuf = (rbuf + 1 == NST) ? 0 : rbuf + 1;
#pragma unroll
    for (int ks = 0; ks < 2; ++ks) {
      s16x8 af[MT], bf[NT];
#pragma unroll
      for (int mt = 0; mt < MT; ++mt) af[mt] = *(const s16x8*)(sb + aoff[ks] + mt * 2048);
#pragma unroll
      for (int nt = 0; nt < NT; ++nt) bf[nt] = tr_frag(sb + boff[nt] + ks * 4096);
#pragma unroll
      for (int mt = 0; mt < MT; ++mt)
#pragma unroll
        for (int nt = 0; nt < NT; ++nt)
          acc[mt][nt] = __builtin_amdgcn_mfma_f32_16x16x32_bf16(bf[nt], af[mt], acc[mt][nt], 0, 0, 0);
    }
  }

  __syncthreads();
#pragma unroll
  for (int mt = 0; mt < MT; ++mt) {
    const int row = wm * MT * 16 + mt * 16 + (lane & 15);
    if (EPI == 2) {
#pragma unroll
      for (int nt = 0; nt < NT / 2; ++nt) {
        const int col = wn * (NT / 2) * 16 + nt * 16 + 4 * (lane >> 4);
        u16 hv[4];
#pragma unroll
        for (int r = 0; r < 4; ++r) {
          const float g = rbf(acc[mt][nt][r]), u = rbf(acc[mt][nt + NT / 2][r]);
          const float sg = rbf(g / (1.0f + expf(-g)));
          hv[r] = f2bf(sg * u);
        }
        u32x2 pk;
        pk[0] = (unsigned)hv[0] | ((unsigned)hv[1] << 16);
        pk[1] = (unsigned)hv[2] | ((unsigned)hv[3] << 16);
        *(u32x2*)(smem + row * CSTRIDE + col * 2) = pk;
      }
    } else {
#pragma unroll
      for (int nt = 0; nt < NT; ++nt) {
        const int col = wn * NT * 16 + nt * 16 + 4 * (lane >> 4);
        u32x2 pk;
        pk[0] = (unsigned)f2bf(acc[mt][nt][0]) | ((unsigned)f2bf(acc[mt][nt][1]) << 16);
        pk[1] = (unsigned)f2bf(acc[mt][nt][2]) | ((unsigned)f2bf(acc[mt][nt][3]) << 16);
        *(u32x2*)(smem + row * CSTRIDE + col * 2) = pk;
      }
    }
  }
  __syncthreads();
  constexpr int CPR = BNO * 2 / 16;
#pragma unroll 4
  for (int idx = tid; idx < BM * CPR; idx += NTH) {
    const int row = idx / CPR, ch = idx % CPR;
    u32x4 v = *(const u32x4*)(smem + row * CSTRIDE + ch * 16);
    const size_t goff = (size_t)(m0 + row) * ldc + n0 + ch * 8;
    if (EPI == 1) {
      u32x4 rv = *(const u32x4*)(R + goff);
#pragma unroll
      for (int i = 0; i < 4; ++i) {
        const float a0 = __uint_as_float(v[i] << 16) + __uint_as_float(rv[i] << 16);
        const float a1 = __uint_as_float(v[i] & 0xffff0000u) + __uint_as_float(rv[i] & 0xffff0000u);
        v[i] = (unsigned)f2bf(a0) | ((unsigned)f2bf(a1) << 16);
      }
    }
    *(u32x4*)(C + goff) = v;
  }
}

extern "C" void kernel_launch(void* const* d_in, const int* in_sizes, int n_in,
                              void* d_out, int out_size, void* d_ws, size_t ws_size,
                              hipStream_t stream) {
  const u16* x0 = (const u16*)d_in[0];
  const u16* wqkv = (const u16*)d_in[1];
  const u16* wo = (const u16*)d_in[2];
  const u16* w1 = (const u16*)d_in[4];
  const u16* w3 = (const u16*)d_in[5];
  const u16* w2 = (const u16*)d_in[6];
  u16* out = (u16*)d_out;
  char* ws = (char*)d_ws;
  u16* xn = (u16*)(ws);
  u16* qkv = (u16*)(ws + (size_t)4 * 1024 * 1024);
  u16* ob = (u16*)(ws + (size_t)16 * 1024 * 1024);
  u16* xa = (u16*)(ws + (size_t)20 * 1024 * 1024);
  u16* xb = (u16*)(ws + (size_t)24 * 1024 * 1024);
  u16* hb = (u16*)(ws + (size_t)28 * 1024 * 1024);

  const u16* xin = x0;
  for (int rep = 0; rep < 2; ++rep) {
    u16* x1 = xa;
    u16* x2 = (rep == 0) ? xb : out;
    ln_k<<<SEQ / 4, 256, 0, stream>>>(xin, xn);
    gemm_k<128, 192, 2, 2, 3, 0, 8, 4, 4><<<256, 256, 0, stream>>>(xn, wqkv, wqkv, nullptr, qkv, DM, 3 * DM, 3 * DM);
    attn_k<<<SEQ * NH * 4 / 256, 256, 0, stream>>>(qkv, ob);
    gemm_k<128, 64, 2, 2, 3, 1, 4, 8, 2><<<256, 256, 0, stream>>>(ob, wo, wo, xin, x1, DM, DM, DM);
    ln_k<<<SEQ / 4, 256, 0, stream>>>(x1, xn);
    gemm_k<256, 256, 2, 4, 2, 2, 4, 7, 4><<<224, 512, 0, stream>>>(xn, w1, w3, nullptr, hb, DM, FF, FF);
    gemm_k<128, 64, 2, 2, 3, 1, 4, 8, 2><<<256, 256, 0, stream>>>(hb, w2, w2, x1, x2, FF, DM, DM);
    xin = x2;
  }
}
```

```cpp
#include <hip/hip_runtime.h>
#include <hip/hip_bf16.h>
#include <stdint.h>

typedef unsigned short u16;
typedef short s16x8 __attribute__((ext_vector_type(8)));
typedef short s16x4 __attribute__((ext_vector_type(4)));
typedef float f32x4 __attribute__((ext_vector_type(4)));
typedef unsigned u32x4 __attribute__((ext_vector_type(4)));
typedef unsigned u32x2 __attribute__((ext_vector_type(2)));
#define LDS_AS __attribute__((address_space(3)))

constexpr int SEQ = 2048, DM = 1024, FF = 3584, NH = 32, DHD = 32;

__device__ __forceinline__ float bf2f(u16 v) { return __uint_as_float(((unsigned)v) << 16); }
__device__ __forceinline__ u16 f2bf(float f) {
  __bf16 b = (__bf16)f;
  return __builtin_bit_cast(u16, b);
}
__device__ __forceinline__ float rbf(float f) { return bf2f(f2bf(f)); }

__global__ __launch_bounds__(256) void ln_k(const u16* __restrict__ x, u16* __restrict__ y) {
  const int row = blockIdx.x * 4 + (threadIdx.x >> 6);
  const int lane = threadIdx.x & 63;
  const u16* xr = x + (size_t)row * DM;
  u32x4 v0 = *(const u32x4*)(xr + lane * 8);
  u32x4 v1 = *(const u32x4*)(xr + 512 + lane * 8);
  float f[16];
#pragma unroll
  for (int i = 0; i < 4; ++i) {
    f[2 * i] = __uint_as_float(v0[i] << 16);
    f[2 * i + 1] = __uint_as_float(v0[i] & 0xffff0000u);
    f[8 + 2 * i] = __uint_as_float(v1[i] << 16);
    f[8 + 2 * i + 1] = __uint_as_float(v1[i] & 0xffff0000u);
  }
  float s = 0.f;
#pragma unroll
  for (int i = 0; i < 16; ++i) s += f[i];
#pragma unroll
  for (int o = 32; o > 0; o >>= 1) s += __shfl_xor(s, o);
  const float mean = s * (1.0f / DM);
  float q = 0.f;
#pragma unroll
  for (int i = 0; i < 16; ++i) { float d = f[i] - mean; q += d * d; }
#pragma unroll
  for (int o = 32; o > 0; o >>= 1) q += __shfl_xor(q, o);
  const float var = q * (1.0f / DM);
  const float mb = rbf(mean);
  const float sd = rbf(sqrtf(rbf(rbf(var) + 1e-5f)));
  u32x4 o0, o1;
#pragma unroll
  for (int i = 0; i < 4; ++i) {
    u16 a = f2bf(rbf(f[2 * i] - mb) / sd), b = f2bf(rbf(f[2 * i + 1] - mb) / sd);
    u16 c = f2bf(rbf(f[8 + 2 * i] - mb) / sd), d = f2bf(rbf(f[8 + 2 * i + 1] - mb) / sd);
    o0[i] = (unsigned)a | ((unsigned)b << 16);
    o1[i] = (unsigned)c | ((unsigned)d << 16);
  }
  u16* yr = y + (size_t)row * DM;
  *(u32x4*)(yr + lane * 8) = o0;
  *(u32x4*)(yr + 512 + lane * 8) = o1;
}

__global__ __launch_bounds__(256) void attn_k(const u16* __restrict__ qkv, u16* __restrict__ o) {
  const int gid = blockIdx.x * 256 + threadIdx.x;
  const int sub = gid & 3, pair = gid >> 2;
  const int h = pair & 31, s = pair >> 5;
  const u16* base = qkv + h * 32 + sub * 8;
  u32x4 qv = *(const u32x4*)(base + (size_t)s * 3072);
  float q[8];
#pragma unroll
  for (int i = 0; i < 4; ++i) { q[2 * i] = __uint_as_float(qv[i] << 16); q[2 * i + 1] = __uint_as_float(qv[i] & 0xffff0000u); }
  float lg[8];
  const float scale = 0.17677669529663687f;
#pragma unroll
  for (int j = 0; j < 8; ++j) {
    const int idx = s - 2 * j;
    float d = 0.f;
    if (idx >= 0) {
      u32x4 kv = *(const u32x4*)(base + (size_t)idx * 3072 + 1024);
#pragma unroll
      for (int i = 0; i < 4; ++i) {
        d += q[2 * i] * __uint_as_float(kv[i] << 16);
        d += q[2 * i + 1] * __uint_as_float(kv[i] & 0xffff0000u);
      }
    }
    d += __shfl_xor(d, 1);
    d += __shfl_xor(d, 2);
    lg[j] = (idx >= 0) ? rbf(rbf(d) * scale) : -1.0e9f;
  }
  float mx = lg[0];
#pragma unroll
  for (int j = 1; j < 8; ++j) mx = fmaxf(mx, lg[j]);
  float e[8], sum = 0.f;
#pragma unroll
  for (int j = 0; j < 8; ++j) { e[j] = (s - 2 * j >= 0) ? expf(lg[j] - mx) : 0.f; sum += e[j]; }
  const float inv = 1.0f / sum;
  float acc[8];
#pragma unroll
  for (int i = 0; i < 8; ++i) acc[i] = 0.f;
#pragma unroll
  for (int j = 0; j < 8; ++j) {
    const int idx = s - 2 * j;
    if (idx >= 0) {
      const float p = rbf(e[j] * inv);
      u32x4 vv = *(const u32x4*)(base + (size_t)idx * 3072 + 2048);
#pragma unroll
      for (int i = 0; i < 4; ++i) {
        acc[2 * i] += p * __uint_as_float(vv[i] << 16);
        acc[2 * i + 1] += p * __uint_as_float(vv[i] & 0xffff0000u);
      }
    }
  }
  u32x4 ov;
#pragma unroll
  for (int i = 0; i < 4; ++i) ov[i] = (unsigned)f2bf(acc[2 * i]) | ((unsigned)f2bf(acc[2 * i + 1]) << 16);
  *(u32x4*)(o + (size_t)s * DM + h * 32 + sub * 8) = ov;
}

typedef float f32x16 __attribute__((ext_vector_type(16)));
__device__ __forceinline__ s16x8 tr_frag(const char* p) {
  s16x4 lo = __builtin_amdgcn_ds_read_tr16_b64_v4i16((LDS_AS s16x4*)(p));
  s16x4 hi = __builtin_amdgcn_ds_read_tr16_b64_v4i16((LDS_AS s16x4*)(p + 512));
  s16x8 r;
  r[0] = lo[0]; r[1] = lo[1]; r[2] = lo[2]; r[3] = lo[3];
  r[4] = hi[0]; r[5] = hi[1]; r[6] = hi[2]; r[7] = hi[3];
  return r;
}

template <int BM, int BN, int WM, int WN, int NL, int NST, int EPI, int PA, int PB, int NPB, int DBG = 0>
__global__ __launch_bounds__((WM * WN + NL) * 64, ((WM * WN + NL) * 64) / 256)
void gemm_k(const u16* __restrict__ A, const u16* __restrict__ B0, const u16* __restrict__ B1,
            const u16* __restrict__ R, u16* __restrict__ C, int K, int ldb, int ldc) {
  constexpr int NW = WM * WN, NTH = NW * 64;
  constexpr int NSW = (NL > 0) ? NL : NW;
  constexpr int MT = BM / (WM * 32), NT = BN / (WN * 32);
  constexpr int ABYTES = BM * 128, BBYTES = BN * 128, SBYTES = ABYTES + BBYTES;
  constexpr int APW = BM / 8 / NSW, BPW = BN / 8 / NSW, PPW = APW + BPW;
  constexpr int BNO = (EPI == 2) ? BN / 2 : BN;
  constexpr int CSTRIDE = BNO * 2 + 16;
  constexpr int SMEM = (NST * SBYTES > BM * CSTRIDE) ? NST * SBYTES : BM * CSTRIDE;
  __shared__ __attribute__((aligned(16))) char smem[SMEM];

  const int tid = threadIdx.x, lane = tid & 63;
  const int wave = __builtin_amdgcn_readfirstlane(tid >> 6);
  const int wm = (wave % NW) / WN, wn = wave % WN;
  const int sw = (NL > 0) ? wave - NW : wave;
  const int bx = blockIdx.x & 7, bi = blockIdx.x >> 3;
  int tm = (bx / NPB) * PA + bi / PB, tn = (bx % NPB) * PB + bi % PB;
  if (DBG == 3) { tm = 0; tn = 0; }
  const int m0 = tm * BM, n0 = tn * BNO;

  unsigned avoff[APW], bvoff[BPW];
#pragma unroll
  for (int j = 0; j < APW; ++j) {
    const int row = (sw * APW + j) * 8 + (lane >> 3);
    avoff[j] = (unsigned)(row * K + (((lane & 7) ^ ((row >> 1) & 7)) << 3)) * 2u;
  }
  const u16* bsel = B0;
  int bcol0 = n0;
  if (EPI == 2 && sw * BPW >= BN / 16) { bsel = B1; bcol0 = n0 - (BN / 2); }
#pragma unroll
  for (int j = 0; j < BPW; ++j) {
    const int i = sw * BPW + j;
    const int nb = i >> 3, kr = ((i & 7) << 3) + (lane >> 3);
    const int chunk = (lane & 7) ^ (((kr >> 1) & 1) << 2);
    bvoff[j] = (unsigned)(kr * ldb + nb * 64 + chunk * 8) * 2u;
  }
  const char* abase = (const char*)(A + (size_t)m0 * K);
  const char* bbase = (const char*)(bsel + bcol0);
  const size_t bstep = (size_t)128 * ldb;
  const unsigned lds0 = (unsigned)(size_t)(LDS_AS char*)smem;

  auto issue_part = [&](int buf, int p0, int p1) {
    if (DBG == 1) return;
    const unsigned sa = lds0 + buf * SBYTES;
#pragma unroll
    for (int j = 0; j < APW; ++j)
      if (j >= p0 && j < p1)
        asm volatile("s_mov_b32 m0, %2\n\ts_nop 0\n\tglobal_load_lds_dwordx4 %0, %1"
                     :: "v"(avoff[j]), "s"(abase), "s"(sa + (sw * APW + j) * 1024) : "memory");
#pragma unroll
    for (int j = 0; j < BPW; ++j)
      if (APW + j >= p0 && APW + j < p1)
        asm volatile("s_mov_b32 m0, %2\n\ts_nop 0\n\tglobal_load_lds_dwordx4 %0, %1"
                     :: "v"(bvoff[j]), "s"(bbase), "s"(sa + ABYTES + (sw * BPW + j) * 1024) : "memory");
  };
  auto advance = [&]() { abase += 128; bbase += bstep; };
  auto issue = [&](int buf) { issue_part(buf, 0, PPW); advance(); };

  int aoff[4];
#pragma unroll
  for (int ks = 0; ks < 4; ++ks) {
    const int row = wm * MT * 32 + (lane & 31);
    aoff[ks] = row * 128 + (((ks * 2 + (lane >> 5)) ^ ((row >> 1) & 7)) << 4);
  }
  int boff[NT];
  {
    const int q = (lane & 15) >> 2, p = lane & 3;
    const int krow = 8 * (lane >> 5) + q;
#pragma unroll
    for (int nt = 0; nt < NT; ++nt) {
      int n32, nb;
      if (EPI == 2) { n32 = wn * (NT / 2) + (nt % (NT / 2)); nb = (nt / (NT / 2)) * (BN / 128) + (n32 >> 1); }
      else { n32 = wn * NT + nt; nb = n32 >> 1; }
      const int chunk = 4 * (n32 & 1) + 2 * ((lane >> 4) & 1) + (p >> 1);
      boff[nt] = ABYTES + nb * 8192 + krow * 128 + ((chunk ^ (((q >> 1) & 1) << 2)) << 4) + ((lane & 1) << 3);
    }
  }

  f32x16 acc[MT][NT];
#pragma unroll
  for (int mt = 0; mt < MT; ++mt)
#pragma unroll
    for (int nt = 0; nt < NT; ++nt)
#pragma unroll
      for (int r = 0; r < 16; ++r) acc[mt][nt][r] = 0.f;

  s16x8 fa[2][MT], fb[2][NT];
  auto rdfrag = [&](int set, const char* sb, int ks) {
#pragma unroll
    for (int mt = 0; mt < MT; ++mt) fa[set][mt] = *(const s16x8*)(sb + aoff[ks] + mt * 4096);
#pragma unroll
    for (int nt = 0; nt < NT; ++nt) fb[set][nt] = tr_frag(sb + boff[nt] + ks * 2048);
  };
  auto mma = [&](int set) {
    if (DBG == 2 || DBG == 3) {
#pragma unroll
      for (int mt = 0; mt < MT; ++mt) asm volatile("" :: "v"(fa[set][mt]));
#pragma unroll
      for (int nt = 0; nt < NT; ++nt) asm volatile("" :: "v"(fb[set][nt]));
      return;
    }
#pragma unroll
    for (int mt = 0; mt < MT; ++mt)
#pragma unroll
      for (int nt = 0; nt < NT; ++nt)
        acc[mt][nt] = __builtin_amdgcn_mfma_f32_32x32x16_bf16(fb[set][nt], fa[set][mt], acc[mt][nt], 0, 0, 0);
  };
  auto sched = [&]() {
    constexpr int RD = MT + 2 * NT, QM = MT * NT;
    constexpr int NP = (RD / 2 < QM) ? RD / 2 : QM;
#pragma unroll
    for (int i = 0; i < NP; ++i) {
      __builtin_amdgcn_sched_group_barrier(0x008, 1, 0);
      __builtin_amdgcn_sched_group_barrier(0x100, (RD + NP - 1) / NP, 0);
    }
    if (QM > NP) __builtin_amdgcn_sched_group_barrier(0x008, QM - NP, 0);
  };
  const int nk = K >> 6;
  if (NL > 0 && wave >= NW) {
#pragma unroll
    for (int s = 0; s < NST - 1; ++s) issue(s);
    int wbuf = NST - 1;
    for (int kt = 0; kt < nk; ++kt) {
      const bool more = (kt + NST - 1 < nk);
      if (kt + NST - 1 <= nk) asm volatile("s_waitcnt vmcnt(%0)" :: "i"((NST - 2) * PPW) : "memory");
      else asm volatile("s_waitcnt vmcnt(0)" ::: "memory");
      __builtin_amdgcn_s_barrier();
      asm volatile("" ::: "memory");
      if (more) issue(wbuf);
      wbuf = (wbuf + 1 == NST) ? 0 : wbuf + 1;
    }
    return;
  }
  if (NL == 0) {
#pragma unroll
    for (int s = 0; s < NST - 1; ++s) issue(s);
    asm volatile("s_waitcnt vmcnt(%0)" :: "i"((NST - 2) * PPW) : "memory");
  }
  __builtin_amdgcn_s_barrier();
  asm volatile("" ::: "memory");
  rdfrag(0, smem, 0);
  int rbuf = 0, wbuf = NST - 1;
  for (int kt = 0; kt < nk - 1; ++kt) {
    const bool more = (kt + NST - 1 < nk);
    const char* sb = smem + rbuf * SBYTES;
    rbuf = (rbuf + 1 == NST) ? 0 : rbuf + 1;
    constexpr int P1 = (PPW + 2) / 3, P2 = (2 * PPW + 2) / 3;
    if (NL == 0 && more) issue_part(wbuf, 0, P1);
    rdfrag(1, sb, 1); mma(0); sched();
    if (NL == 0 && more) issue_part(wbuf, P1, P2);
    rdfrag(0, sb, 2); mma(1); sched();
    if (NL == 0 && more) { issue_part(wbuf, P2, PPW); advance(); }
    rdfrag(1, sb, 3); mma(0); sched();
    if (NL == 0) wbuf = (wbuf + 1 == NST) ? 0 : wbuf + 1;
    if (NL == 0) {
      if (more) asm volatile("s_waitcnt vmcnt(%0)" :: "i"((NST - 2) * PPW) : "memory");
      else asm volatile("s_waitcnt vmcnt(0)" ::: "memory");
    }
    asm volatile("s_waitcnt lgkmcnt(0)" ::: "memory");
    __builtin_amdgcn_s_barrier();
    asm volatile("" ::: "memory");
    rdfrag(0, smem + rbuf * SBYTES, 0); mma(1); sched();
  }
  {
    const char* sb = smem + rbuf * SBYTES;
    rdfrag(1, sb, 1); mma(0); sched();
    rdfrag(0, sb, 2); mma(1); sched();
    rdfrag(1, sb, 3); mma(0); sched();
    mma(1);
  }

  __syncthreads();
#pragma unroll
  for (int mt = 0; mt < MT; ++mt) {
    const int row = wm * MT * 32 + mt * 32 + (lane & 31);
    if (EPI == 2) {
#pragma unroll
      for (int nt = 0; nt < NT / 2; ++nt)
#pragma unroll
        for (int g4 = 0; g4 < 4; ++g4) {
          const int col = wn * (NT / 2) * 32 + nt * 32 + 8 * g4 + 4 * (lane >> 5);
          u16 hv[4];
#pragma unroll
          for (int r = 0; r < 4; ++r) {
            const float g = rbf(acc[mt][nt][4 * g4 + r]), u = rbf(acc[mt][nt + NT / 2][4 * g4 + r]);
            const float sg = rbf(g / (1.0f + expf(-g)));
            hv[r] = f2bf(sg * u);
          }
          u32x2 pk;
          pk[0] = (unsigned)hv[0] | ((unsigned)hv[1] << 16);
          pk[1] = (unsigned)hv[2] | ((unsigned)hv[3] << 16);
          *(u32x2*)(smem + row * CSTRIDE + col * 2) = pk;
        }
    } else {
#pragma unroll
      for (int nt = 0; nt < NT; ++nt)
#pragma unroll
        for (int g4 = 0; g4 < 4; ++g4) {
          const int col = wn * NT * 32 + nt * 32 + 8 * g4 + 4 * (lane >> 5);
          u32x2 pk;
          pk[0] = (unsigned)f2bf(acc[mt][nt][4 * g4 + 0]) | ((unsigned)f2bf(acc[mt][nt][4 * g4 + 1]) << 16);
          pk[1] = (unsigned)f2bf(acc[mt][nt][4 * g4 + 2]) | ((unsigned)f2bf(acc[mt][nt][4 * g4 + 3]) << 16);
          *(u32x2*)(smem + row * CSTRIDE + col * 2) = pk;
        }
    }
  }
  __syncthreads();
  constexpr int CPR = BNO * 2 / 16;
#pragma unroll 4
  for (int idx = tid; idx < BM * CPR; idx += NTH) {
    const int row = idx / CPR, ch = idx % CPR;
    u32x4 v = *(const u32x4*)(smem + row * CSTRIDE + ch * 16);
    const size_t goff = (size_t)(m0 + row) * ldc + n0 + ch * 8;
    if (EPI == 1) {
      u32x4 rv = *(const u32x4*)(R + goff);
#pragma unroll
      for (int i = 0; i < 4; ++i) {
        const float a0 = __uint_as_float(v[i] << 16) + __uint_as_float(rv[i] << 16);
        const float a1 = __uint_as_float(v[i] & 0xffff0000u) + __uint_as_float(rv[i] & 0xffff0000u);
        v[i] = (unsigned)f2bf(a0) | ((unsigned)f2bf(a1) << 16);
      }
    }
    *(u32x4*)(C + goff) = v;
  }
}

extern "C" void kernel_launch(void* const* d_in, const int* in_sizes, int n_in,
                              void* d_out, int out_size, void* d_ws, size_t ws_size,
                              hipStream_t stream) {
  const u16* x0 = (const u16*)d_in[0];
  const u16* wqkv = (const u16*)d_in[1];
  const u16* wo = (const u16*)d_in[2];
  const u16* w1 = (const u16*)d_in[4];
  const u16* w3 = (const u16*)d_in[5];
  const u16* w2 = (const u16*)d_in[6];
  u16* out = (u16*)d_out;
  char* ws = (char*)d_ws;
  u16* xn = (u16*)(ws);
  u16* qkv = (u16*)(ws + (size_t)4 * 1024 * 1024);
  u16* ob = (u16*)(ws + (size_t)16 * 1024 * 1024);
  u16* xa = (u16*)(ws + (size_t)20 * 1024 * 1024);
  u16* xb = (u16*)(ws + (size_t)24 * 1024 * 1024);
  u16* hb = (u16*)(ws + (size_t)28 * 1024 * 1024);

  const u16* xin = x0;
  for (int rep = 0; rep < 2; ++rep) {
    u16* x1 = xa;
    u16* x2 = (rep == 0) ? xb : out;
    ln_k<<<SEQ / 4, 256, 0, stream>>>(xin, xn);
    gemm_k<128, 192, 2, 2, 4, 3, 0, 8, 4, 4><<<256, 512, 0, stream>>>(xn, wqkv, wqkv, nullptr, qkv, DM, 3 * DM, 3 * DM);
    attn_k<<<SEQ * NH * 4 / 256, 256, 0, stream>>>(qkv, ob);
    gemm_k<128, 64, 2, 2, 4, 4, 1, 4, 8, 2><<<256, 512, 0, stream>>>(ob, wo, wo, xin, x1, DM, DM, DM);
    ln_k<<<SEQ / 4, 256, 0, stream>>>(x1, xn);
    gemm_k<256, 256, 2, 4, 0, 2, 2, 4, 7, 4><<<224, 512, 0, stream>>>(xn, w1, w3, nullptr, hb, DM, FF, FF);
    gemm_k<128, 64, 2, 2, 4, 4, 1, 4, 8, 2><<<256, 512, 0, stream>>>(hb, w2, w2, x1, x2, FF, DM, DM);
    xin = x2;
  }
}
```

```cpp
#include <hip/hip_runtime.h>
#include <hip/hip_bf16.h>
#include <stdint.h>

typedef unsigned short u16;
typedef short s16x8 __attribute__((ext_vector_type(8)));
typedef short s16x4 __attribute__((ext_vector_type(4)));
typedef float f32x4 __attribute__((ext_vector_type(4)));
typedef unsigned u32x4 __attribute__((ext_vector_type(4)));
typedef unsigned u32x2 __attribute__((ext_vector_type(2)));
#define LDS_AS __attribute__((address_space(3)))

constexpr int SEQ = 2048, DM = 1024, FF = 3584, NH = 32, DHD = 32;

__device__ __forceinline__ float bf2f(u16 v) { return __uint_as_float(((unsigned)v) << 16); }
__device__ __forceinline__ u16 f2bf(float f) {
  __bf16 b = (__bf16)f;
  return __builtin_bit_cast(u16, b);
}
__device__ __forceinline__ float rbf(float f) { return bf2f(f2bf(f)); }

__global__ __launch_bounds__(256) void ln_k(const u16* __restrict__ x, u16* __restrict__ y) {
  const int row = blockIdx.x * 4 + (threadIdx.x >> 6);
  const int lane = threadIdx.x & 63;
  const u16* xr = x + (size_t)row * DM;
  u32x4 v0 = *(const u32x4*)(xr + lane * 8);
  u32x4 v1 = *(const u32x4*)(xr + 512 + lane * 8);
  float f[16];
#pragma unroll
  for (int i = 0; i < 4; ++i) {
    f[2 * i] = __uint_as_float(v0[i] << 16);
    f[2 * i + 1] = __uint_as_float(v0[i] & 0xffff0000u);
    f[8 + 2 * i] = __uint_as_float(v1[i] << 16);
    f[8 + 2 * i + 1] = __uint_as_float(v1[i] & 0xffff0000u);
  }
  float s = 0.f;
#pragma unroll
  for (int i = 0; i < 16; ++i) s += f[i];
#pragma unroll
  for (int o = 32; o > 0; o >>= 1) s += __shfl_xor(s, o);
  const float mean = s * (1.0f / DM);
  float q = 0.f;
#pragma unroll
  for (int i = 0; i < 16; ++i) { float d = f[i] - mean; q += d * d; }
#pragma unroll
  for (int o = 32; o > 0; o >>= 1) q += __shfl_xor(q, o);
  const float var = q * (1.0f / DM);
  const float mb = rbf(mean);
  const float sd = rbf(sqrtf(rbf(rbf(var) + 1e-5f)));
  u32x4 o0, o1;
#pragma unroll
  for (int i = 0; i < 4; ++i) {
    u16 a = f2bf(rbf(f[2 * i] - mb) / sd), b = f2bf(rbf(f[2 * i + 1] - mb) / sd);
    u16 c = f2bf(rbf(f[8 + 2 * i] - mb) / sd), d = f2bf(rbf(f[8 + 2 * i + 1] - mb) / sd);
    o0[i] = (unsigned)a | ((unsigned)b << 16);
    o1[i] = (unsigned)c | ((unsigned)d << 16);
  }
  u16* yr = y + (size_t)row * DM;
  *(u32x4*)(yr + lane * 8) = o0;
  *(u32x4*)(yr + 512 + lane * 8) = o1;
}

__global__ __launch_bounds__(1024) void attn_k(const u16* __restrict__ qkv, u16* __restrict__ o) {
  constexpr int T = 32, HALO = 14, RS = 528;
  __shared__ __attribute__((aligned(16))) char sm[(2 * (T + HALO) + T) * RS];
  char* sk = sm;
  char* sv = sm + (T + HALO) * RS;
  char* sq = sm + 2 * (T + HALO) * RS;
  const int tid = threadIdx.x;
  const int s0 = (blockIdx.x >> 2) * T, hg = (blockIdx.x & 3) * 8;
  for (int i = tid; i < (2 * (T + HALO) + T) * 32; i += 1024) {
    const int r = i >> 5, c = i & 31;
    int row, sel;
    if (r < T + HALO) { row = s0 - HALO + r; sel = 1; }
    else if (r < 2 * (T + HALO)) { row = s0 - HALO + r - (T + HALO); sel = 2; }
    else { row = s0 + r - 2 * (T + HALO); sel = 0; }
    u32x4 v = (u32x4){0u, 0u, 0u, 0u};
    if (row >= 0) v = *(const u32x4*)(qkv + (size_t)row * 3072 + sel * 1024 + hg * 32 + c * 8);
    *(u32x4*)(sm + r * RS + c * 16) = v;
  }
  __syncthreads();
  const int sub = tid & 3, h = (tid >> 2) & 7, ls = tid >> 5;
  const int s = s0 + ls;
  const int coff = h * 64 + sub * 16;
  float q[8];
  {
    u32x4 v = *(const u32x4*)(sq + ls * RS + coff);
#pragma unroll
    for (int i = 0; i < 4; ++i) { q[2 * i] = __uint_as_float(v[i] << 16); q[2 * i + 1] = __uint_as_float(v[i] & 0xffff0000u); }
  }
  const float scale = 0.17677669529663687f;
  float lg[8];
#pragma unroll
  for (int j = 0; j < 8; ++j) {
    const int lr = ls + HALO - 2 * j;
    u32x4 v = *(const u32x4*)(sk + lr * RS + coff);
    float d = 0.f;
#pragma unroll
    for (int i = 0; i < 4; ++i) {
      d += q[2 * i] * __uint_as_float(v[i] << 16);
      d += q[2 * i + 1] * __uint_as_float(v[i] & 0xffff0000u);
    }
    d += __shfl_xor(d, 1);
    d += __shfl_xor(d, 2);
    lg[j] = (s - 2 * j >= 0) ? rbf(rbf(d) * scale) : -1.0e9f;
  }
  float mx = lg[0];
#pragma unroll
  for (int j = 1; j < 8; ++j) mx = fmaxf(mx, lg[j]);
  float e[8], sum = 0.f;
#pragma unroll
  for (int j = 0; j < 8; ++j) { e[j] = (s - 2 * j >= 0) ? __builtin_amdgcn_exp2f((lg[j] - mx) * 1.44269504088896341f) : 0.f; sum += e[j]; }
  const float inv = 1.0f / sum;
  float acc[8];
#pragma unroll
  for (int i = 0; i < 8; ++i) acc[i] = 0.f;
#pragma unroll
  for (int j = 0; j < 8; ++j) {
    const int lr = ls + HALO - 2 * j;
    const float p = rbf(e[j] * inv);
    u32x4 v = *(const u32x4*)(sv + lr * RS + coff);
#pragma unroll
    for (int i = 0; i < 4; ++i) {
      acc[2 * i] += p * __uint_as_float(v[i] << 16);
      acc[2 * i + 1] += p * __uint_as_float(v[i] & 0xffff0000u);
    }
  }
  u32x4 ov;
#pragma unroll
  for (int i = 0; i < 4; ++i) ov[i] = (unsigned)f2bf(acc[2 * i]) | ((unsigned)f2bf(acc[2 * i + 1]) << 16);
  *(u32x4*)(o + (size_t)s * DM + (hg + h) * 32 + sub * 8) = ov;
}

typedef float f32x16 __attribute__((ext_vector_type(16)));
__device__ __forceinline__ s16x8 tr_frag(const char* p) {
  s16x4 lo = __builtin_amdgcn_ds_read_tr16_b64_v4i16((LDS_AS s16x4*)(p));
  s16x4 hi = __builtin_amdgcn_ds_read_tr16_b64_v4i16((LDS_AS s16x4*)(p + 512));
  s16x8 r;
  r[0] = lo[0]; r[1] = lo[1]; r[2] = lo[2]; r[3] = lo[3];
  r[4] = hi[0]; r[5] = hi[1]; r[6] = hi[2]; r[7] = hi[3];
  return r;
}

template <int BM, int BN, int WM, int WN, int NL, int NST, int EPI, int PA, int PB, int NPB, int DBG = 0>
__global__ __launch_bounds__((WM * WN + NL) * 64, ((WM * WN + NL) * 64) / 256)
void gemm_k(const u16* __restrict__ A, const u16* __restrict__ B0, const u16* __restrict__ B1,
            const u16* __restrict__ R, u16* __restrict__ C, int K, int ldb, int ldc) {
  constexpr int NW = WM * WN, NTH = NW * 64;
  constexpr int NSW = (NL > 0) ? NL : NW;
  constexpr int MT = BM / (WM * 32), NT = BN / (WN * 32);
  constexpr int ABYTES = BM * 128, BBYTES = BN * 128, SBYTES = ABYTES + BBYTES;
  constexpr int APW = BM / 8 / NSW, BPW = BN / 8 / NSW, PPW = APW + BPW;
  constexpr int BNO = (EPI == 2) ? BN / 2 : BN;
  constexpr int CSTRIDE = BNO * 2 + 16;
  constexpr int SMEM = (NST * SBYTES > BM * CSTRIDE) ? NST * SBYTES : BM * CSTRIDE;
  __shared__ __attribute__((aligned(16))) char smem[SMEM];

  const int tid = threadIdx.x, lane = tid & 63;
  const int wave = __builtin_amdgcn_readfirstlane(tid >> 6);
  const int wm = (wave % NW) / WN, wn = wave % WN;
  const int sw = (NL > 0) ? wave - NW : wave;
  const int bx = blockIdx.x & 7, bi = blockIdx.x >> 3;
  int tm = (bx / NPB) * PA + bi / PB, tn = (bx % NPB) * PB + bi % PB;
  if (DBG == 3) { tm = 0; tn = 0; }
  const int m0 = tm * BM, n0 = tn * BNO;

  unsigned avoff[APW], bvoff[BPW];
#pragma unroll
  for (int j = 0; j < APW; ++j) {
    const int row = (sw * APW + j) * 8 + (lane >> 3);
    avoff[j] = (unsigned)(row * K + (((lane & 7) ^ ((row >> 1) & 7)) << 3)) * 2u;
  }
  const u16* bsel = B0;
  int bcol0 = n0;
  if (EPI == 2 && sw * BPW >= BN / 16) { bsel = B1; bcol0 = n0 - (BN / 2); }
#pragma unroll
  for (int j = 0; j < BPW; ++j) {
    const int i = sw * BPW + j;
    const int nb = i >> 3, kr = ((i & 7) << 3) + (lane >> 3);
    const int chunk = (lane & 7) ^ (((kr >> 1) & 1) << 2);
    bvoff[j] = (unsigned)(kr * ldb + nb * 64 + chunk * 8) * 2u;
  }
  const char* abase = (const char*)(A + (size_t)m0 * K);
  const char* bbase = (const char*)(bsel + bcol0);
  const size_t bstep = (size_t)128 * ldb;
  const unsigned lds0 = (unsigned)(size_t)(LDS_AS char*)smem;

  auto issue_part = [&](int buf, int p0, int p1) {
    if (DBG == 1) return;
    const unsigned sa = lds0 + buf * SBYTES;
#pragma unroll
    for (int j = 0; j < APW; ++j)
      if (j >= p0 && j < p1)
        asm volatile("s_mov_b32 m0, %2\n\ts_nop 0\n\tglobal_load_lds_dwordx4 %0, %1"
                     :: "v"(avoff[j]), "s"(abase), "s"(sa + (sw * APW + j) * 1024) : "memory");
#pragma unroll
    for (int j = 0; j < BPW; ++j)
      if (APW + j >= p0 && APW + j < p1)
        asm volatile("s_mov_b32 m0, %2\n\ts_nop 0\n\tglobal_load_lds_dwordx4 %0, %1"
                     :: "v"(bvoff[j]), "s"(bbase), "s"(sa + ABYTES + (sw * BPW + j) * 1024) : "memory");
  };
  auto advance = [&]() { abase += 128; bbase += bstep; };
  auto issue = [&](int buf) { issue_part(buf, 0, PPW); advance(); };

  int aoff[4];
#pragma unroll
  for (int ks = 0; ks < 4; ++ks) {
    const int row = wm * MT * 32 + (lane & 31);
    aoff[ks] = row * 128 + (((ks * 2 + (lane >> 5)) ^ ((row >> 1) & 7)) << 4);
  }
  int boff[NT];
  {
    const int q = (lane & 15) >> 2, p = lane & 3;
    const int krow = 8 * (lane >> 5) + q;
#pragma unroll
    for (int nt = 0; nt < NT; ++nt) {
      int n32, nb;
      if (EPI == 2) { n32 = wn * (NT / 2) + (nt % (NT / 2)); nb = (nt / (NT / 2)) * (BN / 128) + (n32 >> 1); }
      else { n32 = wn * NT + nt; nb = n32 >> 1; }
      const int chunk = 4 * (n32 & 1) + 2 * ((lane >> 4) & 1) + (p >> 1);
      boff[nt] = ABYTES + nb * 8192 + krow * 128 + ((chunk ^ (((q >> 1) & 1) << 2)) << 4) + ((lane & 1) << 3);
    }
  }

  f32x16 acc[MT][NT];
#pragma unroll
  for (int mt = 0; mt < MT; ++mt)
#pragma unroll
    for (int nt = 0; nt < NT; ++nt)
#pragma unroll
      for (int r = 0; r < 16; ++r) acc[mt][nt][r] = 0.f;

  s16x8 fa[2][MT], fb[2][NT];
  auto rdfrag = [&](int set, const char* sb, int ks) {
#pragma unroll
    for (int mt = 0; mt < MT; ++mt) fa[set][mt] = *(const s16x8*)(sb + aoff[ks] + mt * 4096);
#pragma unroll
    for (int nt = 0; nt < NT; ++nt) fb[set][nt] = tr_frag(sb + boff[nt] + ks * 2048);
  };
  auto mma = [&](int set) {
    if (DBG == 2 || DBG == 3) {
#pragma unroll
      for (int mt = 0; mt < MT; ++mt) asm volatile("" :: "v"(fa[set][mt]));
#pragma unroll
      for (int nt = 0; nt < NT; ++nt) asm volatile("" :: "v"(fb[set][nt]));
      return;
    }
#pragma unroll
    for (int mt = 0; mt < MT; ++mt)
#pragma unroll
      for (int nt = 0; nt < NT; ++nt)
        acc[mt][nt] = __builtin_amdgcn_mfma_f32_32x32x16_bf16(fb[set][nt], fa[set][mt], acc[mt][nt], 0, 0, 0);
  };
  auto sched = [&]() {
    constexpr int RD = MT + 2 * NT, QM = MT * NT;
    constexpr int NP = (RD / 2 < QM) ? RD / 2 : QM;
#pragma unroll
    for (int i = 0; i < NP; ++i) {
      __builtin_amdgcn_sched_group_barrier(0x008, 1, 0);
      __builtin_amdgcn_sched_group_barrier(0x100, (RD + NP - 1) / NP, 0);
    }
    if (QM > NP) __builtin_amdgcn_sched_group_barrier(0x008, QM - NP, 0);
  };
  const int nk = K >> 6;
  if (NL > 0 && wave >= NW) {
#pragma unroll
    for (int s = 0; s < NST - 1; ++s) issue(s);
    int wbuf = NST - 1;
    for (int kt = 0; kt < nk; ++kt) {
      const bool more = (kt + NST - 1 < nk);
      if (kt + NST - 1 <= nk) asm volatile("s_waitcnt vmcnt(%0)" :: "i"((NST - 2) * PPW) : "memory");
      else asm volatile("s_waitcnt vmcnt(0)" ::: "memory");
      __builtin_amdgcn_s_barrier();
      asm volatile("" ::: "memory");
      if (more) issue(wbuf);
      wbuf = (wbuf + 1 == NST) ? 0 : wbuf + 1;
    }
    return;
  }
  if (NL == 0) {
#pragma unroll
    for (int s = 0; s < NST - 1; ++s) issue(s);
    asm volatile("s_waitcnt vmcnt(%0)" :: "i"((NST - 2) * PPW) : "memory");
  }
  __builtin_amdgcn_s_barrier();
  asm volatile("" ::: "memory");
  rdfrag(0, smem, 0);
  int rbuf = 0, wbuf = NST - 1;
  for (int kt = 0; kt < nk - 1; ++kt) {
    const bool more = (kt + NST - 1 < nk);
    const char* sb = smem + rbuf * SBYTES;
    rbuf = (rbuf + 1 == NST) ? 0 : rbuf + 1;
    constexpr int P1 = (PPW + 2) / 3, P2 = (2 * PPW + 2) / 3;
    if (NL == 0 && more) issue_part(wbuf, 0, P1);
    rdfrag(1, sb, 1); mma(0); sched();
    if (NL == 0 && more) issue_part(wbuf, P1, P2);
    rdfrag(0, sb, 2); mma(1); sched();
    if (NL == 0 && more) { issue_part(wbuf, P2, PPW); advance(); }
    rdfrag(1, sb, 3); mma(0); sched();
    if (NL == 0) wbuf = (wbuf + 1 == NST) ? 0 : wbuf + 1;
    if (NL == 0) {
      if (more) asm volatile("s_waitcnt vmcnt(%0)" :: "i"((NST - 2) * PPW) : "memory");
      else asm volatile("s_waitcnt vmcnt(0)" ::: "memory");
    }
    asm volatile("s_waitcnt lgkmcnt(0)" ::: "memory");
    __builtin_amdgcn_s_barrier();
    asm volatile("" ::: "memory");
    rdfrag(0, smem + rbuf * SBYTES, 0); mma(1); sched();
  }
  {
    const char* sb = smem + rbuf * SBYTES;
    rdfrag(1, sb, 1); mma(0); sched();
    rdfrag(0, sb, 2); mma(1); sched();
    rdfrag(1, sb, 3); mma(0); sched();
    mma(1);
  }

  if (DBG == 4) {
    float t = 0.f;
#pragma unroll
    for (int mt = 0; mt < MT; ++mt)
#pragma unroll
      for (int nt = 0; nt < NT; ++nt) t += acc[mt][nt][0];
    if (t == 123.456f) C[tid] = 1;
    return;
  }
  __syncthreads();
#pragma unroll
  for (int mt = 0; mt < MT; ++mt) {
    const int row = wm * MT * 32 + mt * 32 + (lane & 31);
    if (EPI == 2) {
#pragma unroll
      for (int nt = 0; nt < NT / 2; ++nt)
#pragma unroll
        for (int g4 = 0; g4 < 4; ++g4) {
          const int col = wn * (NT / 2) * 32 + nt * 32 + 8 * g4 + 4 * (lane >> 5);
          u16 hv[4];
#pragma unroll
          for (int r = 0; r < 4; ++r) {
            const float g = rbf(acc[mt][nt][4 * g4 + r]), u = rbf(acc[mt][nt + NT / 2][4 * g4 + r]);
            const float sg = rbf(g * __builtin_amdgcn_rcpf(1.0f + __builtin_amdgcn_exp2f(-1.44269504088896341f * g)));
            hv[r] = f2bf(sg * u);
          }
          u32x2 pk;
          pk[0] = (unsigned)hv[0] | ((unsigned)hv[1] << 16);
          pk[1] = (unsigned)hv[2] | ((unsigned)hv[3] << 16);
          *(u32x2*)(smem + row * CSTRIDE + col * 2) = pk;
        }
    } else {
#pragma unroll
      for (int nt = 0; nt < NT; ++nt)
#pragma unroll
        for (int g4 = 0; g4 < 4; ++g4) {
          const int col = wn * NT * 32 + nt * 32 + 8 * g4 + 4 * (lane >> 5);
          u32x2 pk;
          pk[0] = (unsigned)f2bf(acc[mt][nt][4 * g4 + 0]) | ((unsigned)f2bf(acc[mt][nt][4 * g4 + 1]) << 16);
          pk[1] = (unsigned)f2bf(acc[mt][nt][4 * g4 + 2]) | ((unsigned)f2bf(acc[mt][nt][4 * g4 + 3]) << 16);
          *(u32x2*)(smem + row * CSTRIDE + col * 2) = pk;
        }
    }
  }
  __syncthreads();
  constexpr int CPR = BNO * 2 / 16;
#pragma unroll 4
  for (int idx = tid; idx < BM * CPR; idx += NTH) {
    const int row = idx / CPR, ch = idx % CPR;
    u32x4 v = *(const u32x4*)(smem + row * CSTRIDE + ch * 16);
    const size_t goff = (size_t)(m0 + row) * ldc + n0 + ch * 8;
    if (EPI == 1) {
      u32x4 rv = *(const u32x4*)(R + goff);
#pragma unroll
      for (int i = 0; i < 4; ++i) {
        const float a0 = __uint_as_float(v[i] << 16) + __uint_as_float(rv[i] << 16);
        const float a1 = __uint_as_float(v[i] & 0xffff0000u) + __uint_as_float(rv[i] & 0xffff0000u);
        v[i] = (unsigned)f2bf(a0) | ((unsigned)f2bf(a1) << 16);
      }
    }
    if (DBG == 5) { if (v[0] == 0x12345) *(u32x4*)(C + goff) = v; }
    else *(u32x4*)(C + goff) = v;
  }
}

extern "C" void kernel_launch(void* const* d_in, const int* in_sizes, int n_in,
                              void* d_out, int out_size, void* d_ws, size_t ws_size,
                              hipStream_t stream) {
  const u16* x0 = (const u16*)d_in[0];
  const u16* wqkv = (const u16*)d_in[1];
  const u16* wo = (const u16*)d_in[2];
  const u16* w1 = (const u16*)d_in[4];
  const u16* w3 = (const u16*)d_in[5];
  const u16* w2 = (const u16*)d_in[6];
  u16* out = (u16*)d_out;
  char* ws = (char*)d_ws;
  u16* xn = (u16*)(ws);
  u16* qkv = (u16*)(ws + (size_t)4 * 1024 * 1024);
  u16* ob = (u16*)(ws + (size_t)16 * 1024 * 1024);
  u16* xa = (u16*)(ws + (size_t)20 * 1024 * 1024);
  u16* xb = (u16*)(ws + (size_t)24 * 1024 * 1024);
  u16* hb = (u16*)(ws + (size_t)28 * 1024 * 1024);

  const u16* xin = x0;
  for (int rep = 0; rep < 2; ++rep) {
    u16* x1 = xa;
    u16* x2 = (rep == 0) ? xb : out;
    ln_k<<<SEQ / 4, 256, 0, stream>>>(xin, xn);
    gemm_k<128, 192, 2, 2, 4, 3, 0, 8, 4, 4><<<256, 512, 0, stream>>>(xn, wqkv, wqkv, nullptr, qkv, DM, 3 * DM, 3 * DM);
    attn_k<<<(SEQ / 32) * 4, 1024, 0, stream>>>(qkv, ob);
    gemm_k<128, 64, 2, 2, 4, 4, 1, 4, 8, 2><<<256, 512, 0, stream>>>(ob, wo, wo, xin, x1, DM, DM, DM);
    ln_k<<<SEQ / 4, 256, 0, stream>>>(x1, xn);
    gemm_k<256, 256, 2, 4, 0, 2, 2, 4, 7, 4><<<224, 512, 0, stream>>>(xn, w1, w3, nullptr, hb, DM, FF, FF);
    gemm_k<128, 64, 2, 2, 4, 4, 1, 4, 8, 2><<<256, 512, 0, stream>>>(hb, w2, w2, x1, x2, FF, DM, DM);
    xin = x2;
  }
}
```
